```python
import torch

def _softmax(x, axis=-1):
    return torch.softmax(x, dim=axis)

def _gelu(x):
    return torch.nn.functional.gelu(x, approximate='tanh')

def _silu(x):
    return torch.nn.functional.silu(x)

def _sigmoid(x):
    return torch.sigmoid(x)

def _layernorm(x, axis=-1, eps=1e-05):
    m = x.mean(dim=axis, keepdim=True)
    v = x.var(dim=axis, keepdim=True, unbiased=False)
    return (x - m) / torch.sqrt(v + eps)

def _scatter_add(tgt, idx, src, axis=0):
    return tgt.clone().index_add_(axis, idx, src)

def _segment_sum(src, seg, n):
    out = torch.zeros((n,) + src.shape[1:], dtype=src.dtype)
    return out.index_add_(0, seg, src)

def _pad(x, pw):
    flat = tuple((v for pair in reversed(pw) for v in pair))
    return torch.nn.functional.pad(x, flat)
kernel_name = 'standalone__tree_speculative_verify_tree_attention_over_candidate_token_tree__decode'

def setup_inputs():

    _w = lambda a: (lambda t: t.to(torch.bfloat16) if t.is_floating_point() else t)(torch.as_tensor(a))
    B, Hq, Hkv, T, C, D = (1, 32, 32, 1, 8192, 128)
    tree_mask = torch.zeros((B, T, T), dtype=float)
    for b in range(B):
        parent = torch.zeros(T, dtype=torch.int64)
        for i in range(1, T):
            parent[i] = int(torch.randint(0, i, (1,)).item())
        for i in range(T):
            j = i
            tree_mask[b, i, j] = 1.0
            while j > 0:
                j = parent[j]
                tree_mask[b, i, j] = 1.0
    return {'q': _w((lambda *s: torch.randn(*s, dtype=torch.bfloat16) * 0.8)(B, Hq, T, D)), 'k_cache': _w((lambda *s: torch.randn(*s, dtype=torch.bfloat16) * 0.8)(B, Hkv, C, D)), 'v_cache': _w((lambda *s: torch.randn(*s, dtype=torch.bfloat16) * 0.05)(B, Hkv, C, D)), 'k_tree': _w((lambda *s: torch.randn(*s, dtype=torch.bfloat16) * 0.8)(B, Hkv, T, D)), 'v_tree': _w((lambda *s: torch.randn(*s, dtype=torch.bfloat16) * 0.05)(B, Hkv, T, D)), 'tree_mask': _w(tree_mask)}

def reference(q, k_cache, v_cache, k_tree, v_tree, tree_mask):
    B, Hq, T, D = q.shape
    Hkv, C = (k_cache.shape[1], k_cache.shape[2])
    rep = Hq // Hkv
    kc = (lambda x, n, axis: torch.repeat_interleave(x, n, dim=axis))(k_cache, rep, axis=1)
    vc = (lambda x, n, axis: torch.repeat_interleave(x, n, dim=axis))(v_cache, rep, axis=1)
    kt = (lambda x, n, axis: torch.repeat_interleave(x, n, dim=axis))(k_tree, rep, axis=1)
    vt = (lambda x, n, axis: torch.repeat_interleave(x, n, dim=axis))(v_tree, rep, axis=1)
    k = torch.concatenate([kc, kt], axis=2)
    v = torch.concatenate([vc, vt], axis=2)
    scale = D ** (-0.5)
    scores = torch.einsum('bhqd,bhkd->bhqk', q, k) * scale
    ctx_mask = torch.ones((B, 1, T, C), dtype=scores.dtype)
    tm = tree_mask[:, None, :, :].clone()
    full_mask = torch.concatenate([ctx_mask, tm], axis=-1)
    scores = scores + (1.0 - full_mask) * -1000000000.0
    p = _softmax(scores, axis=-1)
    return torch.einsum('bhqk,bhkd->bhqd', p, v)
```

```cpp
#include <hip/hip_runtime.h>
#include <hip/hip_bf16.h>
#include <stdint.h>
#include <type_traits>

namespace {

constexpr int H = 32, C = 8192, D = 128;
constexpr int NSPLIT = 8, WAVES = 4;
constexpr int WIN = 6;
constexpr int NT = 2;
constexpr int SC1 = 16;
constexpr unsigned HEAD_BYTES = C * D * 2;
constexpr unsigned CB = 4096;
constexpr unsigned RSTRIDE = NSPLIT * WAVES * CB;
constexpr int R = HEAD_BYTES / RSTRIDE;
static_assert(R == 16 && WIN >= 1 && WIN <= 7, "structure");
constexpr float SCALE = 0.088388347648318440f;
constexpr float LOG2E = 1.4426950408889634f;
constexpr float C2 = SCALE * LOG2E;
constexpr unsigned WS_ML = 256, WS_SLOT = 1024, WS_HEAD = 1024 + NSPLIT * 1024;
constexpr unsigned TAG_STEP = 0x9E3779B8u;
constexpr unsigned OOB = 0x80000000u;
constexpr unsigned SPIN_CAP = 1u << 18;

typedef __attribute__((ext_vector_type(4))) unsigned u32x4;
typedef __attribute__((ext_vector_type(2))) unsigned u32x2;
typedef __attribute__((ext_vector_type(2))) float f32x2;
typedef __attribute__((ext_vector_type(2))) __bf16 bf16x2;

__device__ __forceinline__ float dot2(unsigned a, unsigned b, float acc) {
  return __builtin_amdgcn_fdot2_f32_bf16(__builtin_bit_cast(bf16x2, a), __builtin_bit_cast(bf16x2, b), acc, false);
}
__device__ __forceinline__ float dot8(u32x4 a, u32x4 b) {
  float d = dot2(a.x, b.x, 0.f);
  d = dot2(a.y, b.y, d);
  d = dot2(a.z, b.z, d);
  return dot2(a.w, b.w, d);
}
template <int CTRL> __device__ __forceinline__ float dpp(float x) {
  return __builtin_bit_cast(float, __builtin_amdgcn_mov_dpp(__builtin_bit_cast(int, x), CTRL, 0xf, 0xf, true));
}
constexpr int XOR1 = 0xB1, XOR2 = 0x4E, XOR7 = 0x141, XOR8 = 0x128;
__device__ __forceinline__ float row_sum16(float x) {
  x += dpp<XOR8>(x); x += dpp<XOR7>(x); x += dpp<XOR2>(x); x += dpp<XOR1>(x);
  return x;
}
__device__ __forceinline__ float row_max16(float x) {
  x = fmaxf(x, dpp<XOR8>(x)); x = fmaxf(x, dpp<XOR7>(x)); x = fmaxf(x, dpp<XOR2>(x)); x = fmaxf(x, dpp<XOR1>(x));
  return x;
}
template <int CTRL> __device__ __forceinline__ float trc(float lo, float hi, bool up) {
  const float t1 = lo + dpp<CTRL>(lo), t2 = hi + dpp<CTRL>(hi);
  return up ? t2 : t1;
}
__device__ __forceinline__ float xrow_max(float x) {
  auto s = __builtin_amdgcn_permlane16_swap(__float_as_uint(x), __float_as_uint(x), false, false);
  x = fmaxf(__uint_as_float(s[0]), __uint_as_float(s[1]));
  auto t = __builtin_amdgcn_permlane32_swap(__float_as_uint(x), __float_as_uint(x), false, false);
  return fmaxf(__uint_as_float(t[0]), __uint_as_float(t[1]));
}
__device__ __forceinline__ float xrow_sum(float x) {
  auto s = __builtin_amdgcn_permlane16_swap(__float_as_uint(x), __float_as_uint(x), false, false);
  x = __uint_as_float(s[0]) + __uint_as_float(s[1]);
  auto t = __builtin_amdgcn_permlane32_swap(__float_as_uint(x), __float_as_uint(x), false, false);
  return __uint_as_float(t[0]) + __uint_as_float(t[1]);
}
__device__ __forceinline__ f32x2 unpk(unsigned x) { return f32x2{__uint_as_float(x << 16), __uint_as_float(x & 0xffff0000u)}; }
template <int J> __device__ __forceinline__ float bcast16(float x) { return dpp<0x150 + J>(x); }

template <int N, typename F> __device__ __forceinline__ void static_for(F&& f) {
  if constexpr (N > 0) { static_for<N - 1>(f); f(std::integral_constant<int, N - 1>{}); }
}

__global__ void __launch_bounds__(WAVES * 64, 1)
tree_attn_decode(const unsigned char* __restrict__ q, const unsigned char* __restrict__ kc, const unsigned char* __restrict__ vc,
                 const unsigned char* kt, const unsigned char* vt, const unsigned char* mask,
                 unsigned short* out, unsigned char* ws) {
  __shared__ __attribute__((aligned(16))) float lds[WAVES * 128];
  const unsigned b = blockIdx.x;
  const unsigned h = b / NSPLIT, s = b % NSPLIT;
  const unsigned lane = threadIdx.x & 63;
  const unsigned wave = __builtin_amdgcn_readfirstlane(threadIdx.x >> 6);
  const unsigned r = lane >> 4, c = lane & 15;
  const unsigned DES = h & 7;

  unsigned voff = lane * 16;
  unsigned cbase = (s * WAVES + wave) * CB;
  unsigned r0 = h % R;
  unsigned qoff = (h * 16 + c) * 16;
  asm volatile("" : "+v"(voff), "+s"(cbase), "+s"(r0), "+v"(qoff));
  __builtin_amdgcn_sched_barrier(0);
  const unsigned char* kh = kc + (size_t)h * HEAD_BYTES;
  const unsigned char* vh = vc + (size_t)h * HEAD_BYTES;
  unsigned warm;
  asm volatile("s_load_dword %0, %1, 0x0" : "=s"(warm) : "s"(kh));
  const auto rk = __builtin_amdgcn_make_buffer_rsrc((void*)kh, 0, HEAD_BYTES, 0x00020000);
  const auto rv = __builtin_amdgcn_make_buffer_rsrc((void*)vh, 0, HEAD_BYTES, 0x00020000);
#define SOFF(x) (cbase + (((r0 + (x)) & (R - 1)) * RSTRIDE))

  const u32x4 qv = *reinterpret_cast<const u32x4*>(q + qoff);
  __builtin_amdgcn_sched_barrier(0);
  u32x4 buf[8];
#pragma unroll
  for (int j = 0; j < WIN; ++j) {
    buf[j] = __builtin_amdgcn_raw_buffer_load_b128(rk, voff + (j & 3) * 1024, SOFF(j >> 2), NT);
    __builtin_amdgcn_sched_barrier(0);
  }
  asm volatile("" : "+s"(ws), "+s"(kt), "+s"(vt), "+s"(mask), "+s"(out));
  const unsigned od = wave * 32 + (lane & 31);

#define WAITBUF(j) asm volatile("" :: "v"(buf[(j) & 7]))
#define REFILL(RS, RN, g, j, LAST) do { constexpr int jn_ = (j) + WIN; \
    if constexpr (jn_ < 16) buf[jn_ & 7] = __builtin_amdgcn_raw_buffer_load_b128(RS, voff + (jn_ & 3) * 1024, SOFF(4 * (g) + (jn_ >> 2)), NT); \
    else if constexpr (!(LAST)) buf[jn_ & 7] = __builtin_amdgcn_raw_buffer_load_b128(RN, voff + (jn_ & 3) * 1024, SOFF(4 * (g) + (jn_ >> 2)), NT); \
    __builtin_amdgcn_sched_barrier(0); } while (0)
#define TRC(lo, hi, CTRL, BIT) trc<CTRL>(lo, hi, (c & (BIT)) != 0)
#define KSTEP(RN, g, j) do { \
    WAITBUF(j); \
    REFILL(rk, RN, g, j, false); \
    float x_ = dot8(buf[(j) & 7], qv); \
    if constexpr ((j) & 1) { x_ = TRC(l1, x_, XOR8, 8); \
      if constexpr ((j) & 2) { x_ = TRC(l2, x_, XOR7, 4); \
        if constexpr ((j) & 4) { x_ = TRC(l3, x_, XOR2, 2); \
          if constexpr ((j) & 8) { x_ = TRC(l4, x_, XOR1, 1); sfin = x_; } else l4 = x_; \
        } else l3 = x_; \
      } else l2 = x_; \
    } else l1 = x_; \
    asm volatile("" : "+v"(l1), "+v"(l2), "+v"(l3), "+v"(l4), "+v"(sfin)); } while (0)
#define BITREV4(j) ((((j) & 1) << 3) | (((j) & 2) << 1) | (((j) & 4) >> 1) | (((j) & 8) >> 3))
#define VSTEP(g, j, PREG, LAST) do { \
    WAITBUF(j); \
    REFILL(rv, rv, g, j, LAST); \
    const float pb_ = bcast16<BITREV4(j)>(PREG); \
    const f32x2 p_ = {pb_, pb_}; \
    const u32x4 v_ = buf[(j) & 7]; \
    acc[0] = __builtin_elementwise_fma(p_, unpk(v_.x), acc[0]); \
    acc[1] = __builtin_elementwise_fma(p_, unpk(v_.y), acc[1]); \
    acc[2] = __builtin_elementwise_fma(p_, unpk(v_.z), acc[2]); \
    acc[3] = __builtin_elementwise_fma(p_, unpk(v_.w), acc[3]); \
    asm volatile("" : "+v"(acc[0]), "+v"(acc[1]), "+v"(acc[2]), "+v"(acc[3])); } while (0)
#define PIN1(a) asm volatile("" : "+v"(a))

  float sg0 = 0.f, sg1 = 0.f, sg2 = 0.f, l1 = 0.f, l2 = 0.f, l3 = 0.f, l4 = 0.f, sfin = 0.f;
#pragma unroll 1
  for (unsigned g = 0; g < 3; ++g) {
    static_for<16>([&](auto jc) { constexpr int j = jc.value; KSTEP(rk, g, j); __builtin_amdgcn_sched_barrier(0); });
    sg0 = sg1; sg1 = sg2; sg2 = sfin;
  }
  float Ma, La, tt;
  asm volatile("s_waitcnt lgkmcnt(0)" :: "s"(warm));
  const auto rw = __builtin_amdgcn_make_buffer_rsrc((void*)(ws + (size_t)h * WS_HEAD), 0, WS_HEAD, 0x00020000);
  const bool towner = (s == DES) && (wave == 0);
  const auto rkt = __builtin_amdgcn_make_buffer_rsrc((void*)(kt + h * (D * 2)), 0, towner ? D * 2 : 0, 0x00020000);
  const auto rvt = __builtin_amdgcn_make_buffer_rsrc((void*)(vt + h * (D * 2)), 0, towner ? D * 2 : 0, 0x00020000);
  const auto rmk = __builtin_amdgcn_make_buffer_rsrc((void*)mask, 0, towner ? 4 : 0, 0x00020000);
  const unsigned epoch = __builtin_amdgcn_raw_buffer_load_b32(rw, 0, 0, SC1);
  const u32x4 ktv = __builtin_amdgcn_raw_buffer_load_b128(rkt, c * 16, 0, 0);
  const u32x4 vtv = __builtin_amdgcn_raw_buffer_load_b128(rvt, r == 0 ? c * 16 : OOB, 0, 0);
  const unsigned mk = __builtin_amdgcn_raw_buffer_load_b16(rmk, 0, 0, 0);
  __builtin_amdgcn_sched_barrier(0);
  static_for<16>([&](auto jc) {
    constexpr int j = jc.value;
    KSTEP(rv, 3u, j);
    if constexpr (j == 0) { Ma = row_max16(fmaxf(fmaxf(sg0, sg1), sg2)); PIN1(Ma); }
    if constexpr (j == 2) { Ma = xrow_max(Ma) * C2; PIN1(Ma); }
    if constexpr (j == 4) {
      sg0 = __builtin_amdgcn_exp2f(fmaf(sg0, C2, -Ma)); sg1 = __builtin_amdgcn_exp2f(fmaf(sg1, C2, -Ma));
      sg2 = __builtin_amdgcn_exp2f(fmaf(sg2, C2, -Ma)); La = (sg0 + sg1) + sg2;
      PIN1(sg0); PIN1(sg1); PIN1(sg2); PIN1(La);
    }
    if constexpr (j == 12) {
      const float st = row_sum16(dot8(ktv, qv));
      const float mf = __uint_as_float(mk << 16);
      tt = towner ? (st * SCALE + (1.0f - mf) * -1000000000.0f) * LOG2E : -INFINITY;
      PIN1(tt);
    }
    __builtin_amdgcn_sched_barrier(0);
  });
  const unsigned tag = (epoch + TAG_STEP) | 1u;

  f32x2 acc[4];
#pragma unroll
  for (int k = 0; k < 4; ++k) acc[k] = f32x2{0.f, 0.f};
  float M, L, alpha, sg3 = sfin;
  static_for<16>([&](auto jc) {
    constexpr int j = jc.value;
    VSTEP(0u, j, sg0, false);
    if constexpr (j == 0) { M = row_max16(sg3); PIN1(M); }
    if constexpr (j == 2) { M = fmaxf(fmaxf(xrow_max(M) * C2, Ma), tt); PIN1(M); }
    if constexpr (j == 4) {
      alpha = __builtin_amdgcn_exp2f(Ma - M); sg3 = __builtin_amdgcn_exp2f(fmaf(sg3, C2, -M));
      L = fmaf(La, alpha, sg3); PIN1(alpha); PIN1(sg3); PIN1(L);
    }
    if constexpr (j == 6) { L = row_sum16(L); PIN1(L); }
    if constexpr (j == 8) { tt = __builtin_amdgcn_exp2f(tt - M); L = xrow_sum(L) + tt; PIN1(L); PIN1(tt); }
    if constexpr (j == 15) {
      const u32x2 gr = {__float_as_uint(lane ? L : M), tag};
      __builtin_amdgcn_raw_buffer_store_b64(gr, rw, lane < 2 ? WS_ML + ((s * WAVES + wave) * 2 + lane) * 8 : OOB, 0, SC1);
    }
    __builtin_amdgcn_sched_barrier(0);
  });
#pragma unroll 1
  for (unsigned g = 1; g < 3; ++g) {
    static_for<16>([&](auto jc) { constexpr int j = jc.value; VSTEP(g, j, sg1, false); __builtin_amdgcn_sched_barrier(0); });
    sg1 = sg2;
  }
  u32x2 ml = __builtin_amdgcn_raw_buffer_load_b64(rw, WS_ML + lane * 8, 0, SC1);
  {
    const f32x2 al = {alpha, alpha}, tw = {tt, tt};
    acc[0] = __builtin_elementwise_fma(tw, unpk(vtv.x), acc[0] * al);
    acc[1] = __builtin_elementwise_fma(tw, unpk(vtv.y), acc[1] * al);
    acc[2] = __builtin_elementwise_fma(tw, unpk(vtv.z), acc[2] * al);
    acc[3] = __builtin_elementwise_fma(tw, unpk(vtv.w), acc[3] * al);
  }
  float fscale;
  static_for<16>([&](auto jc) {
    constexpr int j = jc.value;
    VSTEP(3u, j, sg3, true);
    if constexpr (j == 16 - WIN) {
      if (__builtin_expect(!__all(ml.y == tag), 0)) {
        for (unsigned spins = 0; spins < SPIN_CAP; ++spins) {
          __builtin_amdgcn_s_sleep(2);
          asm volatile("" ::: "memory");
          ml = __builtin_amdgcn_raw_buffer_load_b64(rw, WS_ML + lane * 8, 0, SC1);
          if (__all(ml.y == tag)) break;
        }
      }
      const float x = __uint_as_float(ml.x);
      const float mw = dpp<0xA0>(x);
      const float Mg = xrow_max(row_max16(mw));
      const float e = (lane & 1) ? x * __builtin_amdgcn_exp2f(mw - Mg) : 0.f;
      fscale = __builtin_amdgcn_exp2f(M - Mg) / xrow_sum(row_sum16(e));
      PIN1(fscale);
      __builtin_amdgcn_raw_buffer_store_b32(tag, rw, (s == DES && threadIdx.x == 0) ? 0u : OOB, 0, SC1);
    }
    __builtin_amdgcn_sched_barrier(0);
  });

  float z[4], w2[2];
#pragma unroll
  for (int k = 0; k < 4; ++k) {
    auto t = __builtin_amdgcn_permlane32_swap(__float_as_uint(acc[k >> 1][k & 1]), __float_as_uint(acc[2 + (k >> 1)][k & 1]), false, false);
    z[k] = __uint_as_float(t[0]) + __uint_as_float(t[1]);
  }
#pragma unroll
  for (int k = 0; k < 2; ++k) {
    auto t = __builtin_amdgcn_permlane16_swap(__float_as_uint(z[k]), __float_as_uint(z[k + 2]), false, false);
    w2[k] = (__uint_as_float(t[0]) + __uint_as_float(t[1])) * fscale;
  }
  *reinterpret_cast<f32x2*>(lds + wave * 128 + 8 * c + 2 * r) = f32x2{w2[0], w2[1]};
  __syncthreads();
  const float own = (lds[od] + lds[128 + od]) + (lds[256 + od] + lds[384 + od]);
  if (s != DES) {
    const u32x2 gr = {__float_as_uint(own), tag};
    __builtin_amdgcn_raw_buffer_store_b64(gr, rw, lane < 32 ? WS_SLOT + s * 1024 + od * 8 : OOB, 0, SC1);
    return;
  }
  const unsigned half = lane >> 5;
  float val[4];
  unsigned off[4];
#pragma unroll
  for (int i = 0; i < 4; ++i) {
    const unsigned sp = 2 * i + half;
    const bool mine = (sp == DES);
    val[i] = mine ? own : 0.f;
    off[i] = mine ? OOB : WS_SLOT + sp * 1024 + od * 8;
  }
  const unsigned long long wsa = (unsigned long long)(ws + (size_t)h * WS_HEAD), oa = (unsigned long long)(out + h * D);
  const u32x4 dws = {(unsigned)wsa, (unsigned)(wsa >> 32) & 0xffffu, WS_HEAD, 0x00020000u};
  const u32x4 dout = {(unsigned)oa, (unsigned)(oa >> 32) & 0xffffu, (unsigned)(D * 2), 0x00020000u};
  const unsigned ooff = lane < 32 ? od * 2 : OOB;
  unsigned long long m0, m1, m2, m3;
  unsigned t, cnt;
#define PI(B0, B1, B2, B3, B4, B5, B6, B7) \
  "buffer_load_dwordx2 v[" #B0 ":" #B1 "], %[o0], %[dws], 0 offen sc1\n\t" \
  "buffer_load_dwordx2 v[" #B2 ":" #B3 "], %[o1], %[dws], 0 offen sc1\n\t" \
  "buffer_load_dwordx2 v[" #B4 ":" #B5 "], %[o2], %[dws], 0 offen sc1\n\t" \
  "buffer_load_dwordx2 v[" #B6 ":" #B7 "], %[o3], %[dws], 0 offen sc1\n\t"
#define PC(B0, B1, B2, B3, B4, B5, B6, B7) \
  "s_waitcnt vmcnt(8)\n\t" \
  "v_cmp_eq_u32_e64 %[m0], %[tag], v" #B1 "\n\t" \
  "v_cmp_eq_u32_e64 %[m1], %[tag], v" #B3 "\n\t" \
  "v_cmp_eq_u32_e64 %[m2], %[tag], v" #B5 "\n\t" \
  "v_cmp_eq_u32_e64 %[m3], %[tag], v" #B7 "\n\t" \
  "v_cndmask_b32_e64 %[v0], %[v0], v" #B0 ", %[m0]\n\t" \
  "v_cndmask_b32_e64 %[o0], %[o0], %[oob], %[m0]\n\t" \
  "v_cndmask_b32_e64 %[v1], %[v1], v" #B2 ", %[m1]\n\t" \
  "v_cndmask_b32_e64 %[o1], %[o1], %[oob], %[m1]\n\t" \
  "v_cndmask_b32_e64 %[v2], %[v2], v" #B4 ", %[m2]\n\t" \
  "v_cndmask_b32_e64 %[o2], %[o2], %[oob], %[m2]\n\t" \
  "v_cndmask_b32_e64 %[v3], %[v3], v" #B6 ", %[m3]\n\t" \
  "v_cndmask_b32_e64 %[o3], %[o3], %[oob], %[m3]\n\t" \
  "v_and_b32_e32 %[t], %[o0], %[o1]\n\t" \
  "v_and_b32_e32 %[t], %[t], %[o2]\n\t" \
  "v_and_b32_e32 %[t], %[t], %[o3]\n\t" \
  "v_cmp_le_i32_e32 vcc, 0, %[t]\n\t" \
  "s_nop 1\n\t" \
  "s_cbranch_vccz .Lpoll_done%=\n\t"
#define SET0(M) M(96, 97, 98, 99, 100, 101, 102, 103)
#define SET1(M) M(104, 105, 106, 107, 108, 109, 110, 111)
#define SET2(M) M(112, 113, 114, 115, 116, 117, 118, 119)
  asm volatile(
      "s_nop 4\n\t"
      SET0(PI) "s_sleep 4\n\t" SET1(PI) "s_sleep 4\n\t" SET2(PI)
      "s_mov_b32 %[cnt], %[cap]\n"
      ".Lpoll_loop%=:\n\t"
      SET0(PC) SET0(PI) SET1(PC) SET1(PI) SET2(PC) SET2(PI)
      "s_sub_u32 %[cnt], %[cnt], 1\n\t"
      "s_cbranch_scc0 .Lpoll_loop%=\n"
      ".Lpoll_done%=:\n\t"
      "v_add_f32_e32 %[v0], %[v0], %[v1]\n\t"
      "v_add_f32_e32 %[v2], %[v2], %[v3]\n\t"
      "v_add_f32_e32 %[v0], %[v0], %[v2]\n\t"
      "v_mov_b32_e32 %[v1], %[v0]\n\t"
      "s_nop 1\n\t"
      "v_permlane32_swap_b32_e32 %[v0], %[v1]\n\t"
      "s_nop 1\n\t"
      "v_add_f32_e32 %[v0], %[v0], %[v1]\n\t"
      "v_cvt_pk_bf16_f32 %[v0], %[v0], %[v0]\n\t"
      "buffer_store_short %[v0], %[ooff], %[dout], 0 offen sc0 sc1\n\t"
      "s_waitcnt vmcnt(0)"
      : [v0] "+&v"(val[0]), [v1] "+&v"(val[1]), [v2] "+&v"(val[2]), [v3] "+&v"(val[3]),
        [o0] "+&v"(off[0]), [o1] "+&v"(off[1]), [o2] "+&v"(off[2]), [o3] "+&v"(off[3]),
        [m0] "=&s"(m0), [m1] "=&s"(m1), [m2] "=&s"(m2), [m3] "=&s"(m3), [t] "=&v"(t), [cnt] "=&s"(cnt)
      : [tag] "v"(tag), [oob] "v"(OOB), [dws] "s"(dws), [dout] "s"(dout), [ooff] "v"(ooff), [cap] "i"(SPIN_CAP)
      : "memory", "vcc", "scc",
        "v96", "v97", "v98", "v99", "v100", "v101", "v102", "v103", "v104", "v105", "v106", "v107",
        "v108", "v109", "v110", "v111", "v112", "v113", "v114", "v115", "v116", "v117", "v118", "v119");
}

}

extern "C" void kernel_launch(void* const* d_in, const int* in_sizes, int n_in, void* d_out, int out_size,
                              void* d_ws, size_t ws_size, hipStream_t stream) {
  (void)in_sizes; (void)n_in; (void)out_size; (void)ws_size;
  tree_attn_decode<<<H * NSPLIT, WAVES * 64, 0, stream>>>(
      (const unsigned char*)d_in[0], (const unsigned char*)d_in[1], (const unsigned char*)d_in[2], (const unsigned char*)d_in[3],
      (const unsigned char*)d_in[4], (const unsigned char*)d_in[5], (unsigned short*)d_out, (unsigned char*)d_ws);
}
```
